```python
import torch

def _softmax(x, axis=-1):
    return torch.softmax(x, dim=axis)
def _gelu(x):
    return torch.nn.functional.gelu(x, approximate="tanh")
def _silu(x):
    return torch.nn.functional.silu(x)
def _sigmoid(x):
    return torch.sigmoid(x)
def _layernorm(x, axis=-1, eps=1e-5):
    m = x.mean(dim=axis, keepdim=True)
    v = x.var(dim=axis, keepdim=True, unbiased=False)
    return (x - m) / torch.sqrt(v + eps)
def _scatter_add(tgt, idx, src, axis=0):
    return tgt.clone().index_add_(axis, idx, src)
def _segment_sum(src, seg, n):
    out = torch.zeros((n,) + src.shape[1:], dtype=src.dtype)
    return out.index_add_(0, seg, src)
def _pad(x, pw):
    flat = tuple(v for pair in reversed(pw) for v in pair)
    return torch.nn.functional.pad(x, flat)

kernel_name = 'standalone__paged_block_table_kv_pagedattention__sampling'

def setup_inputs():

    _w = (lambda a: (lambda t: t.to(torch.bfloat16) if t.is_floating_point() else t)(torch.as_tensor(a)))
    B, Hq, Hkv, Sq, Skv, D = 8, 32, 32, 1, 2048, 128
    bs = 16
    nbps = max(1, Skv // bs)
    num_phys = B * nbps + 4
    bt = torch.randperm(num_phys)[: B * nbps].reshape(B, nbps).to(torch.int64)
    cl = (nbps * bs - (torch.arange(1, B + 1, dtype=torch.int64) * 2654435761) % max(1, nbps * bs // 2)).to(torch.int64)
    return {
        "q": _w((lambda *s: torch.randn(*s, dtype=torch.bfloat16) * 0.8)(B, Hq, Sq, D)),
        "k_cache": _w((lambda *s: torch.randn(*s, dtype=torch.bfloat16) * 0.8)(num_phys, bs, Hkv, D)),
        "v_cache": _w((lambda *s: torch.randn(*s, dtype=torch.bfloat16) * 0.05)(num_phys, bs, Hkv, D)),
        "block_table": _w(bt),
        "context_lens": _w(cl),
    }

def reference(q, k_cache, v_cache, block_table, context_lens):
    B, Hq, Sq, D = q.shape
    bs = k_cache.shape[1]
    Hkv = k_cache.shape[2]
    nbps = block_table.shape[1]
    Skv = nbps * bs
    bt = block_table.reshape(-1)
    k = (lambda x, *d: x.permute(*d))(k_cache[bt].reshape(B, Skv, Hkv, D), 0, 2, 1, 3)
    v = (lambda x, *d: x.permute(*d))(v_cache[bt].reshape(B, Skv, Hkv, D), 0, 2, 1, 3)
    g = Hq // Hkv
    qg = q.reshape(B, Hkv, g, Sq, D)
    scores = torch.einsum("bhgqd,bhkd->bhgqk", qg, k) * (D ** -0.5)
    pos = torch.arange(Skv)
    msk = pos[None, :] < context_lens[:, None]
    scores = torch.where(msk[:, None, None, None, :], scores, -1e9)
    p = _softmax(scores, axis=-1)
    out = torch.einsum("bhgqk,bhkd->bhgqd", p, v)
    return out.reshape(B, Hq, Sq, D)
```

```cpp
#include <hip/hip_runtime.h>
#include <hip/hip_bf16.h>
#include <stdint.h>

constexpr int B = 8, H = 32, D = 128, PAGE = 16, NBPS = 128, SKV = NBPS * PAGE;
constexpr int NWS = 8, NT = (NWS + 1) * 64, RING = 3;
constexpr int NEW = 9, NE = NWS * NEW;
constexpr int TOK_BYTES = H * D * 2;
constexpr float C2 = 0.08838834764831845f * 1.4426950408889634f;
constexpr float MASKED_T = -998244352.0f * 1.4426950408889634f;
constexpr float NEG_BIG = -3.0e38f;
constexpr unsigned OOB = 0x80000000u;
constexpr int NT_AUX = 2;
constexpr int PREC = 132;
constexpr int NDW = 68;
constexpr unsigned D_FD = 1u << 15, D_FL = 1u << 14, D_V = 1u << 13, D_LIVE = 0xf0u, D_LOW = 0xffffu;
constexpr unsigned tau_of(double gbs) { return (unsigned)(2048.0 * 256 / (gbs * 1e9) * 1e8 * 256 + 0.5); }
constexpr unsigned TAUQ = tau_of(7700), TAUM = tau_of(7920);
constexpr unsigned VSLOW = 15;
constexpr unsigned TOLQ = 9 * 256;
constexpr int CODE_PF = 32;
constexpr unsigned TLB_STRIDE = 16u << 20;
constexpr size_t WS_PAD = 4096, WS_SLOT = 16384;
static_assert(NEW % RING == 0 && NEW <= 16, "early window");

using u32x4 = __attribute__((ext_vector_type(4))) unsigned;
using u32x2 = __attribute__((ext_vector_type(2))) unsigned;
using f32x2 = __attribute__((ext_vector_type(2))) float;
using f32x4 = __attribute__((ext_vector_type(4))) float;
using bf16x2 = __attribute__((ext_vector_type(2))) __bf16;
typedef __attribute__((address_space(1))) unsigned gu32;
typedef __attribute__((address_space(1))) unsigned long long gu64;

__device__ __forceinline__ float dot2(unsigned k, unsigned q, float acc) {
  return __builtin_amdgcn_fdot2_f32_bf16(__builtin_bit_cast(bf16x2, k), __builtin_bit_cast(bf16x2, q), acc, false);
}
template <int CTRL> __device__ __forceinline__ float dpp(float x) {
  return __builtin_bit_cast(float, __builtin_amdgcn_mov_dpp(__builtin_bit_cast(int, x), CTRL, 0xf, 0xf, true));
}
__device__ __forceinline__ float row_sum16(float x) {
  x += dpp<0xB1>(x); x += dpp<0x4E>(x); x += dpp<0x141>(x); x += dpp<0x140>(x);
  return x;
}
__device__ __forceinline__ float x32_max(float x) {
  auto t = __builtin_amdgcn_permlane32_swap(__float_as_uint(x), __float_as_uint(x), false, false);
  return fmaxf(__uint_as_float(t[0]), __uint_as_float(t[1]));
}
__device__ __forceinline__ float x16_max(float x) {
  auto t = __builtin_amdgcn_permlane16_swap(__float_as_uint(x), __float_as_uint(x), false, false);
  return fmaxf(__uint_as_float(t[0]), __uint_as_float(t[1]));
}
__device__ __forceinline__ float swap32_add(float a, float b) {
  auto t = __builtin_amdgcn_permlane32_swap(__float_as_uint(a), __float_as_uint(b), false, false);
  return __uint_as_float(t[0]) + __uint_as_float(t[1]);
}
__device__ __forceinline__ float swap16_add(float a, float b) {
  auto t = __builtin_amdgcn_permlane16_swap(__float_as_uint(a), __float_as_uint(b), false, false);
  return __uint_as_float(t[0]) + __uint_as_float(t[1]);
}
__device__ __forceinline__ float bflo(unsigned u) { return __uint_as_float(u << 16); }
__device__ __forceinline__ float bfhi(unsigned u) { return __uint_as_float(u & 0xffff0000u); }
__device__ __forceinline__ unsigned pack_bf16(float a, float b) {
  const __hip_bfloat16 r0 = __float2bfloat16(a), r1 = __float2bfloat16(b);
  return (unsigned)__builtin_bit_cast(unsigned short, r0) | ((unsigned)__builtin_bit_cast(unsigned short, r1) << 16);
}
__device__ __forceinline__ unsigned f23(float v) { const unsigned u = __float_as_uint(v); return (u + 0xffu + ((u >> 9) & 1u)) >> 9; }
__device__ __forceinline__ unsigned now_q() { return (unsigned)__builtin_amdgcn_s_memrealtime() << 8; }
__device__ __forceinline__ float ex2(float x) { return __builtin_amdgcn_exp2f(x); }

__global__ void __launch_bounds__(NT, 5) paged_attn_kernel(const unsigned short* __restrict__ q, const unsigned short* __restrict__ kc,
                                                       const unsigned short* __restrict__ vc, const int* __restrict__ bt,
                                                       const int* __restrict__ lens, unsigned short* __restrict__ out,
                                                       unsigned char* __restrict__ ws, unsigned kv_bytes) {
  constexpr int LDS_BT = 512, LDS_DESC = LDS_BT + 1024, LDS_SC = LDS_DESC + NWS * NDW, LDS_PART = LDS_SC + NWS * 256, NREC = (B + 1) * NWS,
                LDS_CNT = LDS_PART + NREC * PREC, LDS_ALL = LDS_CNT + 4;
  __shared__ __attribute__((aligned(16))) float lds[LDS_ALL];
  unsigned* lq = reinterpret_cast<unsigned*>(lds);
  int* lbt = reinterpret_cast<int*>(lds + LDS_BT);
  unsigned* ldesc = reinterpret_cast<unsigned*>(lds + LDS_DESC);
  float* part = lds + LDS_PART;
  unsigned* lcnt = reinterpret_cast<unsigned*>(lds + LDS_CNT);
  const int bo = blockIdx.x % B, h = blockIdx.x / B;
  const int tid = threadIdx.x, lane = tid & 63, w = __builtin_amdgcn_readfirstlane(tid >> 6), r = lane >> 4, c = lane & 15;
  unsigned pf;
  {
    unsigned long long pc; unsigned clen;
    asm volatile("s_getpc_b64 %0\n.Lpa_code_pc:\n\ts_mov_b32 %1, .Lpa_code_end-.Lpa_code_pc" : "=s"(pc), "=s"(clen));
    pf = *(const __attribute__((address_space(1))) unsigned*)(pc + min((unsigned)((tid >> 6) * CODE_PF + (lane & (CODE_PF - 1))) * 64u, (unsigned)max((int)clen, 0)));
  }
  lcnt[lane & 3] = 0u;
  asm volatile("" :: "s"(q), "s"(kc), "s"(vc), "s"(bt), "s"(lens), "s"(out), "s"(ws), "s"(kv_bytes) : "memory");
  const __amdgpu_buffer_rsrc_t krs = __builtin_amdgcn_make_buffer_rsrc((void*)kc, 0, kv_bytes, 0x00020000);
  const __amdgpu_buffer_rsrc_t vrs = __builtin_amdgcn_make_buffer_rsrc((void*)vc, 0, kv_bytes, 0x00020000);
  const unsigned tof = (lane == 0 ? (unsigned)w * TLB_STRIDE : OOB) + (unsigned)bo * 1280u;
  const unsigned tk = __builtin_amdgcn_raw_buffer_load_b32(krs, tof, 0, NT_AUX);
  const unsigned tv = __builtin_amdgcn_raw_buffer_load_b32(vrs, tof, 0, NT_AUX);
  __builtin_amdgcn_sched_barrier(0);
  const int leno_raw = *(w == NWS ? reinterpret_cast<const int*>(out + (bo * H + h) * D) : lens + bo);
  unsigned desc = (unsigned)(w == NWS ? reinterpret_cast<const int*>(ws + WS_PAD + bo * 1280) : bt + bo * NBPS + (w >> 1))[4 * c];
  u32x4 qv = *reinterpret_cast<const u32x4*>(q + (bo * H + h) * D + 8 * c);
  __builtin_amdgcn_sched_barrier(0);
  asm volatile("s_waitcnt lgkmcnt(0)" : "+v"(desc) : "v"(pf), "v"(tk), "v"(tv) : "memory");
  __builtin_amdgcn_s_barrier();
  __builtin_amdgcn_sched_barrier(0);
  const unsigned org = now_q();
  const unsigned voff = (unsigned)(r * TOK_BYTES + h * (D * 2) + c * 16);
#define LDS_ADD(i) do { __builtin_amdgcn_fence(__ATOMIC_RELEASE, "workgroup"); \
    if (lane == 0) __hip_atomic_fetch_add(lcnt + (i), 1u, __ATOMIC_RELAXED, __HIP_MEMORY_SCOPE_WORKGROUP); } while (0)
#define LDS_WAIT(i, n, slp) do { \
    while (__builtin_amdgcn_readfirstlane(__hip_atomic_load(lcnt + (i), __ATOMIC_RELAXED, __HIP_MEMORY_SCOPE_WORKGROUP)) < (unsigned)(n)) __builtin_amdgcn_s_sleep(slp); \
    __builtin_amdgcn_fence(__ATOMIC_ACQUIRE, "workgroup"); } while (0)

  if (__builtin_expect(w >= NWS, 0)) {
    __builtin_amdgcn_s_setprio(2);
    const __attribute__((address_space(4))) int* lens4 = (const __attribute__((address_space(4))) int*)lens;
    int len[B];
#pragma unroll
    for (int i = 0; i < B; ++i) len[i] = lens4[i];
    const unsigned* qs = reinterpret_cast<const unsigned*>(q) + (r * H + h) * (D / 2) + 4 * c;
    u32x4 tb[4];
#pragma unroll
    for (int i = 0; i < 4; ++i) tb[i] = *reinterpret_cast<const u32x4*>(bt + 256 * i + 4 * lane);
    const u32x4 q0 = *reinterpret_cast<const u32x4*>(qs), q1 = *reinterpret_cast<const u32x4*>(qs + 4 * H * (D / 2));
    gu32* ep = (gu32*)ws + (lane & 7) * H + h;
    const unsigned epv = __hip_atomic_load(ep, __ATOMIC_RELAXED, __HIP_MEMORY_SCOPE_AGENT);
    __builtin_amdgcn_sched_barrier(0);
    for (int i = lane; i < NREC; i += 64) part[i * PREC + 2] = 0.f;
    int U = 0;
#pragma unroll
    for (int i = 0; i < B; ++i) { len[i] = min(max(len[i], 0), SKV); U += max((len[i] + 7) >> 3, NE); }
    const int W = (U + 7) >> 3;
    int PX[B + 1], PY[B + 1];
    PX[0] = 0; PY[0] = 0;
#pragma unroll
    for (int i = 0; i < B; ++i) { const int d = max((len[i] + 7) >> 3, NE) - W; PX[i + 1] = PX[i] + max(d, 0); PY[i + 1] = PY[i] + max(-d, 0); }
    const int XT = PX[B];
    int leno = 0, px0 = 0, px1 = 0, f0 = 0, f1 = 0;
#pragma unroll
    for (int i = 0; i < B; ++i) if (i == bo) { leno = len[i]; px0 = PX[i]; px1 = PX[i + 1]; f0 = min(PY[i], XT); f1 = min(PY[i + 1], XT); }
    const int F = f1 - f0, cnt = F + min(max((leno + 7) >> 3, NE), W);
#pragma unroll
    for (int i = 0; i < 4; ++i) *reinterpret_cast<u32x4*>(lbt + 256 * i + 4 * lane) = tb[i];
    *reinterpret_cast<u32x4*>(lq + 4 * lane) = q0;
    *reinterpret_cast<u32x4*>(lq + 256 + 4 * lane) = q1;
    {
      const int wv = lane & 7, nw = (cnt - wv + NWS - 1) / NWS, nf = max(F - wv + NWS - 1, 0) / NWS;
      unsigned* dw = ldesc + wv * NDW - NEW;
#pragma nounroll
      for (int i = lane >> 3; i < 36; i += 8) {
        const int n = wv + NWS * i, x = f0 + n - NE;
        int sb = 0, off = W, lb = len[0], xe = PX[1];
#pragma unroll
        for (int s = 1; s < B; ++s) { const bool ge = x >= PX[s]; sb = ge ? s : sb; off = ge ? W - PX[s] : off; lb = ge ? len[s] : lb; xe = ge ? PX[s + 1] : xe; }
        const bool own = n < NE || n >= NE + F;
        sb = own ? bo : sb; off = own ? (n < NE ? NE - f0 : NE - f1) : off; lb = own ? leno : lb;
        const int tok = 8 * (off + x);
        const int nv = min(max(lb - tok, 0), 8);
        const int ph = lbt[sb * NBPS + min(tok >> 4, NBPS - 1)];
        const unsigned d = (unsigned)ph * (unsigned)(PAGE * TOK_BYTES) + ((unsigned)((tok & 15) * TOK_BYTES) | (unsigned)sb | ((unsigned)nv << 4) | ((unsigned)i << 8));
        if (i < nw) {
          if (i >= NEW) dw[i] = d | ((wv == 0 && i < NEW + RING) ? D_FL : 0u);
          const int k = i < NEW ? i + nf : (own ? i : i - NEW);
          dw[nw + k] = d | D_V | (own ? (k == nw - 1 ? D_FL : 0u) : (x + NWS >= f1 ? D_FL | D_FD : (x + NWS >= xe ? D_FL : 0u)));
        } else {
          dw[2 * i] = 0u; dw[2 * i + 1] = 0u;
        }
      }
      if (lane < NWS) *reinterpret_cast<u32x2*>(ldesc + lane * NDW + 64) =
          u32x2{(unsigned)(2 * nw), (unsigned)((float)(2 * cnt - NE) * (float)TAUQ / (float)(2 * nw - NEW))};
    }
    LDS_ADD(2);
    asm volatile("" :: "v"(desc), "v"(qv), "v"(leno_raw));
    const unsigned tagv = epv + 1u;
    float M = NEG_BIG, L = 0.f, o0 = 0.f, o1 = 0.f;
    unsigned tm = 0, sm = 0;
#pragma unroll
    for (int s = 0; s < B; ++s) {
      if (max(f0, PX[s]) < min(f1, PX[s + 1])) tm |= 1u << s;
      if (max(min(PY[s], XT), px0) < min(min(PY[s + 1], XT), px1)) sm |= 1u << s;
    }
    if (sm) {
      const unsigned tag = (unsigned)__builtin_amdgcn_readlane((int)tagv, bo);
      int fk = 0;
#pragma unroll
      for (int s = 0; s < B; ++s) if (sm >> s & 1u) fk = max(fk, min(PY[s + 1], XT) - min(PY[s], XT));
      const unsigned due = org + (unsigned)min(W + fk + 44, 2 * W - 44) * TAUQ;
      while ((int)(now_q() - due) < 0) __builtin_amdgcn_s_sleep(8);
      const gu64* g = (const gu64*)(ws + WS_SLOT) + (bo * B * H + h) * 64 + lane;
#pragma nounroll
      for (unsigned spin = 0; sm && spin < (1u << 22); ++spin) {
        unsigned long long x[3];
        unsigned s2 = sm;
#pragma unroll
        for (int i = 0; i < 3; ++i) { x[i] = __hip_atomic_load(g + __builtin_ctz(s2 ? s2 : sm) * (H * 64), __ATOMIC_RELAXED, __HIP_MEMORY_SCOPE_AGENT); s2 &= s2 - 1; }
        bool go = true;
#pragma unroll
        for (int i = 0; i < 3; ++i) {
          const unsigned lo = (unsigned)x[i], hi = (unsigned)(x[i] >> 32);
          const unsigned long long bal = __ballot((hi >> 14) & 1u);
          go = go && sm && __all((hi >> 15) == (tag & 0x1ffffu)) && (unsigned)(bal >> 32) == tag;
          if (go) {
            const float mr = __uint_as_float((unsigned)bal), Mn = fmaxf(M, mr), e0 = ex2(M - Mn), e1 = ex2(mr - Mn);
            M = Mn; L = L * e0 + e1;
            o0 = o0 * e0 + __uint_as_float(lo << 9) * e1; o1 = o1 * e0 + __uint_as_float((hi << 18) | ((lo >> 23) << 9)) * e1;
            sm &= sm - 1;
          }
        }
        if (!go && sm) __builtin_amdgcn_s_sleep(8);
      }
      if (lane == bo) __hip_atomic_store(ep, tagv, __ATOMIC_RELAXED, __HIP_MEMORY_SCOPE_AGENT);
    }
    unsigned short* op = out + (bo * H + h) * D;
    tm |= (1u << bo) | (1u << B);
    bool redo = false;
#pragma nounroll
    for (; tm; tm &= tm - 1) {
      const int t = __builtin_ctz(tm);
      const bool fin = t == B, mine = t == bo || fin;
      if (fin) LDS_WAIT(1, NWS, 1); else LDS_WAIT(0, min(F, NWS), 2);
      const float* rec = part + t * NWS * PREC;
      f32x4 hd[NWS]; f32x2 a[NWS];
#pragma unroll
      for (int i = 0; i < NWS; ++i) { hd[i] = *reinterpret_cast<const f32x4*>(rec + i * PREC); a[i] = *reinterpret_cast<const f32x2*>(rec + i * PREC + 4 + 2 * lane); }
      const float Ms = mine ? M : NEG_BIG;
      float Mt = Ms;
#pragma unroll
      for (int i = 0; i < NWS; ++i) { hd[i][0] = hd[i][2] != 0.f ? hd[i][0] : NEG_BIG; Mt = fmaxf(Mt, hd[i][0]); }
      const float e0 = mine ? ex2(Ms - Mt) : 0.f;
      float Lt = L * e0, p0 = o0 * e0, p1 = o1 * e0;
#pragma unroll
      for (int i = 0; i < NWS; ++i) {
        const bool ok = hd[i][2] != 0.f; const float e = ex2(hd[i][0] - Mt);
        Lt = fmaf(e, ok ? hd[i][1] : 0.f, Lt); p0 = fmaf(e, ok ? a[i][0] : 0.f, p0); p1 = fmaf(e, ok ? a[i][1] : 0.f, p1);
      }
      const float inv = __builtin_amdgcn_rcpf(Lt);
      const unsigned pk = pack_bf16(p0 * inv, p1 * inv);
      if (fin) {
        redo = !(Mt > MASKED_T + 256.f);
        *reinterpret_cast<unsigned*>(op + 2 * lane) = pk;
      } else if (t != bo) {
        const unsigned tg = (unsigned)__builtin_amdgcn_readlane((int)tagv, t);
        const unsigned lse = __float_as_uint(Mt + __builtin_amdgcn_logf(Lt));
        const unsigned u0 = f23(p0 * inv), u1 = f23(p1 * inv), sp = ((lane < 32 ? lse : tg) >> (lane & 31)) & 1u;
        const unsigned long long gr = (unsigned long long)(u0 | (u1 << 23)) | ((unsigned long long)((u1 >> 9) | (sp << 14) | (tg << 15)) << 32);
        __hip_atomic_store((gu64*)(ws + WS_SLOT) + ((t * B + bo) * H + h) * 64 + lane, gr, __ATOMIC_RELAXED, __HIP_MEMORY_SCOPE_AGENT);
      }
    }
    if (__builtin_expect(__builtin_amdgcn_readfirstlane(redo), 0)) {
      asm volatile(".Lpa_code_end:");
      const u32x4 q2 = *reinterpret_cast<const u32x4*>(lq + bo * 64 + 4 * c);
      float m1 = NEG_BIG, l1 = 0.f, b8[8];
#pragma unroll
      for (int j = 0; j < 8; ++j) b8[j] = 0.f;
#pragma nounroll
      for (int t = 0; t < SKV; t += 4) {
        const unsigned off = (unsigned)(lbt[bo * NBPS + (t >> 4)] * PAGE + (t & 15)) * (unsigned)TOK_BYTES + voff;
        const u32x4 k1 = __builtin_amdgcn_raw_buffer_load_b128(krs, off, 0, 0), v1 = __builtin_amdgcn_raw_buffer_load_b128(vrs, off, 0, 0);
        float s1 = dot2(k1[0], q2[0], 0.f); s1 = dot2(k1[1], q2[1], s1); s1 = dot2(k1[2], q2[2], s1); s1 = dot2(k1[3], q2[3], s1);
        s1 = row_sum16(s1);
        const float t1 = t + r < leno ? s1 * C2 : MASKED_T;
        const float mn = fmaxf(m1, t1), al = ex2(m1 - mn), pp = ex2(t1 - mn);
        m1 = mn; l1 = fmaf(l1, al, pp);
#pragma unroll
        for (int j = 0; j < 4; ++j) { b8[2 * j] = fmaf(pp, bflo(v1[j]), b8[2 * j] * al); b8[2 * j + 1] = fmaf(pp, bfhi(v1[j]), b8[2 * j + 1] * al); }
      }
      const float mw = x32_max(x16_max(m1)), s1 = ex2(m1 - mw);
      float lw = l1 * s1; lw = swap16_add(lw, lw); lw = swap32_add(lw, lw);
      float b4[4], b2[2];
#pragma unroll
      for (int j = 0; j < 4; ++j) b4[j] = swap32_add(b8[j] * s1, b8[j + 4] * s1);
#pragma unroll
      for (int j = 0; j < 2; ++j) b2[j] = swap16_add(b4[j], b4[j + 2]);
      const float inv = 1.f / lw;
      *reinterpret_cast<unsigned*>(op + 8 * c + 2 * r) = pack_bf16(b2[0] * inv, b2[1] * inv);
    }
    return;
  }
  {
    u32x4 X[RING], Y[RING];
    unsigned sd[RING];
    desc = desc * (unsigned)(PAGE * TOK_BYTES) + (unsigned)(8 * (w & 1) * TOK_BYTES + bo) +
           ((unsigned)min(max(max(leno_raw, 0) - 8 * (w + NWS * c), 0), 8) << 4) + ((unsigned)c << 8);
    unsigned nxt = org + (unsigned)w * TAUQ, sched = nxt, fast = nxt, tend = nxt + 0x40000000u, per = NWS * TAUQ;
#define ISSUE(slot, it) do { const unsigned d_ = (unsigned)__builtin_amdgcn_readlane((int)desc, (it)); sd[slot] = d_; \
    const int nv_ = (int)((d_ >> 4) & 15u); const unsigned vo_ = r < nv_ ? voff : OOB, vp_ = r + 4 < nv_ ? voff + 4 * TOK_BYTES : OOB; \
    const __amdgpu_buffer_rsrc_t rs_ = __builtin_amdgcn_make_buffer_rsrc((void*)((d_ & D_V) ? vc : kc), 0, kv_bytes, 0x00020000); \
    if (d_ & D_LIVE) { unsigned now_ = now_q(); while ((int)(now_ - nxt) < 0) { __builtin_amdgcn_s_sleep(1); now_ = now_q(); } \
      sched += (d_ & D_V) ? per + per / 1024 * VSLOW : per - per / 1024 * VSLOW; \
      const unsigned e_ = now_ - TOLQ; fast = ((int)(e_ - fast) > 0 ? e_ : fast) + NWS * TAUM; \
      nxt = ((int)(fast - sched) > 0 && (int)(now_ - tend) < 0) ? fast : sched; } \
    X[slot] = __builtin_amdgcn_raw_buffer_load_b128(rs_, vo_, d_ & ~D_LOW, NT_AUX); \
    Y[slot] = __builtin_amdgcn_raw_buffer_load_b128(rs_, vp_, d_ & ~D_LOW, NT_AUX); __builtin_amdgcn_sched_barrier(0); } while (0)
    __builtin_amdgcn_sched_barrier(0);
#pragma unroll
    for (int s = 0; s < RING; ++s) ISSUE(s, s);
    float m = NEG_BIG, l = 0.f;
    f32x2 acc[4];
#pragma unroll
    for (int j = 0; j < 4; ++j) acc[j] = f32x2{0.f, 0.f};
    int curq = bo;
    float* lsc = lds + LDS_SC + w * 256 + 2 * r;
#define FLUSH(seg) do { \
    const float mw_ = x32_max(x16_max(m)), sc_ = ex2(m - mw_); \
    float lw_ = l * sc_; lw_ = swap16_add(lw_, lw_); lw_ = swap32_add(lw_, lw_); \
    float a4_[4], a2_[2]; \
    _Pragma("unroll") for (int j_ = 0; j_ < 4; ++j_) a4_[j_] = swap32_add(acc[j_ >> 1][j_ & 1] * sc_, acc[2 + (j_ >> 1)][j_ & 1] * sc_); \
    _Pragma("unroll") for (int j_ = 0; j_ < 2; ++j_) a2_[j_] = swap16_add(a4_[j_], a4_[j_ + 2]); \
    float* rec_ = part + ((seg) * NWS + w) * PREC; \
    *reinterpret_cast<f32x2*>(rec_ + 4 + 8 * c + 2 * r) = f32x2{a2_[0], a2_[1]}; \
    if (lane == 0) *reinterpret_cast<f32x4*>(rec_) = f32x4{mw_, lw_, 1.f, 0.f}; } while (0)
#define PIN() asm volatile("" : "+v"(acc[0]), "+v"(acc[1]), "+v"(acc[2]), "+v"(acc[3]), "+v"(m), "+v"(l) :: "memory")
#define COMPUTE(slot) do { const unsigned d_ = sd[slot]; \
    if ((d_ & (D_V | D_LIVE)) > D_V) {                               \
      const f32x2 t_ = *reinterpret_cast<const f32x2*>(lsc + ((d_ >> 5) & 0xf8u)); \
      const float mn_ = fmaxf(fmaxf(t_[0], t_[1]), m), al_ = ex2(m - mn_), p0_ = ex2(t_[0] - mn_), p1_ = ex2(t_[1] - mn_); \
      m = mn_; l = fmaf(l, al_, p0_ + p1_); \
      _Pragma("unroll") for (int j_ = 0; j_ < 4; ++j_) { \
        acc[j_] = __builtin_elementwise_fma(f32x2{p0_, p0_}, f32x2{bflo(X[slot][j_]), bfhi(X[slot][j_])}, acc[j_] * al_); \
        acc[j_] = __builtin_elementwise_fma(f32x2{p1_, p1_}, f32x2{bflo(Y[slot][j_]), bfhi(Y[slot][j_])}, acc[j_]); } \
    } \
    PIN(); \
    if (__builtin_expect(d_ & D_FL, 0)) { const int sb_ = (int)(d_ & 7u); FLUSH((d_ & D_V) ? (sb_ == bo ? B : sb_) : bo); \
      if (d_ & D_FD) LDS_ADD(0); \
      if ((d_ & D_V) && sb_ == bo) LDS_ADD(1); \
      m = NEG_BIG; l = 0.f; _Pragma("unroll") for (int j_ = 0; j_ < 4; ++j_) acc[j_] = f32x2{0.f, 0.f}; } \
    PIN(); \
    if ((d_ & (D_V | D_LIVE)) - 1u < D_V - 1u) {                     \
      if (__builtin_expect((int)(d_ & 7u) != curq, 0)) { curq = (int)(d_ & 7u); qv = *reinterpret_cast<const u32x4*>(lq + curq * 64 + 4 * c); } \
      float s_ = dot2(X[slot][0], qv[0], 0.f), u_ = dot2(Y[slot][0], qv[0], 0.f); \
      _Pragma("unroll") for (int j_ = 1; j_ < 4; ++j_) { s_ = dot2(X[slot][j_], qv[j_], s_); u_ = dot2(Y[slot][j_], qv[j_], u_); } \
      s_ = row_sum16(s_); asm volatile("" : "+v"(s_)); u_ = row_sum16(u_); const int nv_ = (int)((d_ >> 4) & 15u); \
      *reinterpret_cast<f32x2*>(lsc + ((d_ >> 5) & 0xf8u)) = f32x2{r < nv_ ? s_ * C2 : MASKED_T, r + 4 < nv_ ? u_ * C2 : MASKED_T}; \
    } \
    asm volatile("" :: "v"(X[slot]), "v"(Y[slot]) : "memory"); \
    __builtin_amdgcn_sched_barrier(0); } while (0)

    __builtin_amdgcn_sched_barrier(0);
    int ib = RING, left = -1;
    do {
      if (__builtin_expect(left < 0 && ib == NEW, 0)) {
        LDS_WAIT(2, 1, 1);
        desc = ldesc[w * NDW + lane];
        const u32x2 pr = *reinterpret_cast<const u32x2*>(ldesc + w * NDW + 64);
        const int ns = __builtin_amdgcn_readfirstlane((int)pr[0]);
        left = (ns + RING - 1) / RING - (NEW / RING - 1);
        per = __builtin_amdgcn_readfirstlane(pr[1]);
        tend = sched + (unsigned)(ns - NEW) * per;
        ib = 0;
      }
#pragma unroll
      for (int s = 0; s < RING; ++s) { COMPUTE(s); ISSUE(s, ib + s); }
      ib += RING;
    } while (--left != 0);
#pragma unroll
    for (int s = 0; s < RING; ++s) asm volatile("" :: "v"(X[s]), "v"(Y[s]));
  }
}

extern "C" void kernel_launch(void* const* d_in, const int* in_sizes, int n_in, void* d_out, int out_size, void* d_ws, size_t ws_size,
                              hipStream_t stream) {
  const unsigned kv_bytes = (unsigned)in_sizes[1] * 2u;
  paged_attn_kernel<<<B * H, NT, 0, stream>>>((const unsigned short*)d_in[0], (const unsigned short*)d_in[1], (const unsigned short*)d_in[2],
                                             (const int*)d_in[3], (const int*)d_in[4], (unsigned short*)d_out, (unsigned char*)d_ws, kv_bytes);
}
```
